```python
import torch

def _softmax(x, axis=-1):
    return torch.softmax(x, dim=axis)
def _gelu(x):
    return torch.nn.functional.gelu(x, approximate="tanh")
def _silu(x):
    return torch.nn.functional.silu(x)
def _sigmoid(x):
    return torch.sigmoid(x)
def _layernorm(x, axis=-1, eps=1e-5):
    m = x.mean(dim=axis, keepdim=True)
    v = x.var(dim=axis, keepdim=True, unbiased=False)
    return (x - m) / torch.sqrt(v + eps)
def _scatter_add(tgt, idx, src, axis=0):
    return tgt.clone().index_add_(axis, idx, src)
def _segment_sum(src, seg, n):
    out = torch.zeros((n,) + src.shape[1:], dtype=src.dtype)
    return out.index_add_(0, seg, src)
def _pad(x, pw):
    flat = tuple(v for pair in reversed(pw) for v in pair)
    return torch.nn.functional.pad(x, flat)

kernel_name = 'standalone__gqa_block_cached__near_ridge'

def setup_inputs():

    _w = (lambda a: (lambda t: t.to(torch.bfloat16) if t.is_floating_point() else t)(torch.as_tensor(a)))
    B, Sq, D = 8, 240, 512
    Hq, Hkv, Sk, Dh = 32, 32, 512, 128
    assert Hq % Hkv == 0, (Hq, Hkv)
    return {
        "x": _w((lambda *s: torch.randn(*s, dtype=torch.bfloat16) * 0.8)(B, Sq, D)),
        "k_cache": _w((lambda *s: torch.randn(*s, dtype=torch.bfloat16) * 0.8)(B, Hkv, Sk, Dh)),
        "v_cache": _w((lambda *s: torch.randn(*s, dtype=torch.bfloat16) * 0.05)(B, Hkv, Sk, Dh)),
        "wq": _w((lambda *s: torch.randn(*s, dtype=torch.bfloat16) * 0.8)(D, Hq * Dh) * (D ** -0.5)),
        "wo": _w((lambda *s: torch.randn(*s, dtype=torch.bfloat16) * 0.05)(Hq * Dh, D) * ((Hq * Dh) ** -0.5)),
    }

def reference(x, k_cache, v_cache, wq, wo):
    B, Sq, D = x.shape
    Hq, Dh = 32, 128
    Hkv = k_cache.shape[1]
    rep = Hq // Hkv
    q = (lambda x, *d: x.permute(*d))((x @ wq).reshape(B, Sq, Hq, Dh), 0, 2, 1, 3)
    k = (lambda x, n, axis: torch.repeat_interleave(x, n, dim=axis))(k_cache, rep, axis=1)
    v = (lambda x, n, axis: torch.repeat_interleave(x, n, dim=axis))(v_cache, rep, axis=1)
    scores = torch.einsum("bhqd,bhkd->bhqk", q, k) * (Dh ** -0.5)
    p = _softmax(scores, axis=-1)
    o = torch.einsum("bhqk,bhkd->bhqd", p, v)
    return (lambda x, *d: x.permute(*d))(o, 0, 2, 1, 3).reshape(B, Sq, Hq * Dh) @ wo
```

```cpp
#include <hip/hip_runtime.h>
#include <hip/hip_bf16.h>
#include <utility>

typedef __bf16 bf16x8 __attribute__((ext_vector_type(8)));
typedef __bf16 bf16x2 __attribute__((ext_vector_type(2)));
typedef short s16x4 __attribute__((ext_vector_type(4)));
typedef short s16x8 __attribute__((ext_vector_type(8)));
typedef float f32x2 __attribute__((ext_vector_type(2)));
typedef float f32x4 __attribute__((ext_vector_type(4)));
typedef float f32x16 __attribute__((ext_vector_type(16)));
typedef unsigned u32x4 __attribute__((ext_vector_type(4)));
#define LDSP(T) __attribute__((address_space(3))) T*

constexpr int NB = 8, SQ = 240, DM = 512, NH = 32, SK = 512, DH = 128;
constexpr float QSCALE = 0.088388347648318440f * 1.4426950408889634f;
constexpr float THR = 8.0f;

#define STAMP_DECL
#define STAMP(i)
#define STAMPC(i)
#define STAMPL(b, v)
#undef TT
#define STAMP_OUT(a, b)

#define STT(i)
#define STAMP2(i) STAMP(i)
#define SBAR() __builtin_amdgcn_sched_barrier(0)
#define WAITV(n) asm volatile("s_waitcnt vmcnt(" #n ")" ::: "memory")
#define WAITL0() asm volatile("s_waitcnt lgkmcnt(0)" ::: "memory")
#define BARRIER() asm volatile("s_barrier" ::: "memory")

template <int OFF>
__device__ __forceinline__ void dma(unsigned lds, const void* base, unsigned voff) {
  asm volatile("s_mov_b32 m0, %0\n\ts_nop 0\n\tglobal_load_lds_dwordx4 %1, %2 offset:%3"
               :: "s"(lds - OFF), "v"(voff), "s"(base), "i"(OFF) : "memory");
}
#define NTS ""
template <int OFF>
__device__ __forceinline__ void dmas(unsigned lds, const void* base, unsigned voff) {
  asm volatile("s_mov_b32 m0, %0\n\ts_nop 0\n\tglobal_load_lds_dwordx4 %1, %2 offset:%3" NTS
               :: "s"(lds - OFF), "v"(voff), "s"(base), "i"(OFF) : "memory");
}
__device__ __forceinline__ bf16x8 ld128(unsigned a) { return *(LDSP(bf16x8))a; }
__device__ __forceinline__ bf16x8 ldtr2(unsigned a0, unsigned a1) {
  s16x4 lo = __builtin_amdgcn_ds_read_tr16_b64_v4i16((LDSP(s16x4))a0);
  s16x4 hi = __builtin_amdgcn_ds_read_tr16_b64_v4i16((LDSP(s16x4))a1);
  s16x8 r = __builtin_shufflevector(lo, hi, 0, 1, 2, 3, 4, 5, 6, 7);
  return __builtin_bit_cast(bf16x8, r);
}
__device__ __forceinline__ unsigned pk2(float a, float b) {
  f32x2 v = {a, b};
  return __builtin_bit_cast(unsigned, __builtin_convertvector(v, bf16x2));
}
template <int S>
__device__ __forceinline__ bf16x8 pack8(const f32x16& x) {
  u32x4 w = {pk2(x[8 * S + 0], x[8 * S + 1]), pk2(x[8 * S + 2], x[8 * S + 3]), pk2(x[8 * S + 4], x[8 * S + 5]), pk2(x[8 * S + 6], x[8 * S + 7])};
  return __builtin_bit_cast(bf16x8, w);
}
__device__ __forceinline__ float max3(float a, float b, float c) {
  float r; asm("v_max3_f32 %0, %1, %2, %3" : "=v"(r) : "v"(a), "v"(b), "v"(c)); return r;
}
__device__ __forceinline__ float fsub(float a, float b) {
  float r; asm("v_sub_f32 %0, %1, %2" : "=v"(r) : "v"(a), "v"(b)); return r;
}
__device__ __forceinline__ float fmul(float a, float b) {
  float r; asm("v_mul_f32 %0, %1, %2" : "=v"(r) : "v"(a), "v"(b)); return r;
}
__device__ __forceinline__ void exp4(float a, float b, float c, float d, float& l0, float& l1, float& l2, float& l3, u32x4& w, int half) {
  unsigned p0, p1; float e1, e3;
  asm volatile("v_exp_f32 %0, %8\n\tv_exp_f32 %1, %9\n\tv_exp_f32 %2, %10\n\tv_exp_f32 %3, %11\n\t"
               "v_add_f32 %4, %4, %0\n\tv_add_f32 %5, %5, %1\n\tv_cvt_pk_bf16_f32 %0, %0, %1\n\t"
               "v_add_f32 %6, %6, %2\n\tv_add_f32 %7, %7, %3\n\tv_cvt_pk_bf16_f32 %2, %2, %3"
               : "=&v"(p0), "=&v"(e1), "=&v"(p1), "=&v"(e3), "+v"(l0), "+v"(l1), "+v"(l2), "+v"(l3) : "v"(a), "v"(b), "v"(c), "v"(d));
  w[2 * half] = p0; w[2 * half + 1] = p1;
}
template <class T> __device__ __forceinline__ void pins(T& a) { asm volatile("" : "+s"(a)); }
__device__ __forceinline__ float xhalf(float x) {
  auto rr = __builtin_amdgcn_permlane32_swap(__float_as_uint(x), __float_as_uint(x), false, false);
  return __uint_as_float((threadIdx.x & 32) ? rr[0] : rr[1]);
}
__device__ __forceinline__ void OPAQUE2(unsigned& a, unsigned& b) { asm volatile("" : "+v"(a), "+v"(b)); }
constexpr int PD = 4;
#define MFMA(a, b, c) __builtin_amdgcn_mfma_f32_32x32x16_bf16(a, b, c, 0, 0, 0)

template <int V> struct IC { static constexpr int value = V; };
template <class F, int... I> __device__ __forceinline__ void static_for_impl(F&& f, std::integer_sequence<int, I...>) { (f(IC<I>{}), ...); }
template <int N, class F> __device__ __forceinline__ void static_for(F&& f) { static_for_impl(f, std::make_integer_sequence<int, N>{}); }
template <int OFF>
__device__ __forceinline__ void st_sc1(const void* base, unsigned voff, bf16x8 v) {
  asm volatile("global_store_dwordx4 %0, %1, %2 offset:%3 sc1\n\ts_nop 1" :: "v"(voff), "v"(v), "s"(base), "i"(OFF) : "memory");
}
__device__ __forceinline__ void ld_sc1(bf16x8& d, const void* base, unsigned voff) {
  asm volatile("global_load_dwordx4 %0, %1, %2 sc1" : "=v"(d) : "v"(voff), "s"(base) : "memory");
}
__device__ __forceinline__ void ldw_sc1(unsigned& d, const void* base, unsigned voff) {
  asm volatile("global_load_dword %0, %1, %2 sc1" : "=v"(d) : "v"(voff), "s"(base) : "memory");
}
__device__ __forceinline__ void stw_sc1(const void* base, unsigned voff, unsigned v) {
  asm volatile("global_store_dword %0, %1, %2 sc1" :: "v"(voff), "v"(v), "s"(base) : "memory");
}
template <int N> __device__ __forceinline__ void waitv2(bf16x8& a, bf16x8& b) { asm volatile("s_waitcnt vmcnt(%2)" : "+v"(a), "+v"(b) : "i"(N) : "memory"); }
template <int N> __device__ __forceinline__ void waitv1(unsigned& a) { asm volatile("s_waitcnt vmcnt(%1)" : "+v"(a) : "i"(N) : "memory"); }
__device__ __forceinline__ void pin4(bf16x8& a, bf16x8& b, bf16x8& c, bf16x8& d) { asm volatile("" : "+v"(a), "+v"(b), "+v"(c), "+v"(d) :: "memory"); }
__device__ __forceinline__ void pin2(bf16x8& a, bf16x8& b) { asm volatile("" : "+v"(a), "+v"(b) :: "memory"); }
#define APRIO 1
#define PUBN 0
constexpr int seq(int n) { return n < 8 ? n : n < 12 ? n + 10 : n < 22 ? n - 4 : n; }
constexpr int issued(int n) { const int k = n == 0 ? 4 : n < 6 ? 2 * n + 2 : n + 8; return k > 32 ? 32 : k; }
constexpr int inflight(int n) {
  int first = 0; while (issued(first) <= n) ++first;
  int wo = 0; for (int m = first; m < n; ++m) { if (m >= 8 && m < 12) wo += 2; if (m == 14 || m == 24) wo += 1; }
  return 2 * (issued(n) - n - 1) + wo;
}
#define DMAWO dma
#define CODELINES 119
__device__ __forceinline__ void touch(unsigned lds, const void* base, unsigned voff) {
  asm volatile("s_mov_b32 m0, %0\n\ts_nop 0\n\tglobal_load_lds_dword %1, %2" :: "s"(lds), "v"(voff), "s"(base) : "memory");
}
constexpr unsigned RBS = NH * 8192 + 4096;
#define OUTPOL " sc1"
constexpr unsigned U = 16384;
constexpr unsigned KUNITS = 0x09734097u;
constexpr unsigned long long WUNITS = 0x1608957324ull;
constexpr unsigned AUNITS = 0x87409734u;
constexpr unsigned BUNITS = 0x05216852u;
constexpr unsigned VUNITS = 0x16852168u;

__global__ __launch_bounds__(512, 2) void attn_kernel(const __bf16* __restrict__ x, const __bf16* __restrict__ kc, const __bf16* __restrict__ vc,
                                                      const __bf16* __restrict__ wq, const __bf16* __restrict__ wo, __bf16* __restrict__ out,
                                                      char* ofrag, unsigned* sync) {
  extern __shared__ __attribute__((aligned(16))) char smem[];
  const unsigned lds0 = (unsigned)(size_t)(LDSP(char))smem;
  const int lane = threadIdx.x & 63;
  const int w = __builtin_amdgcn_readfirstlane(threadIdx.x >> 6);
  const int b = (blockIdx.x & 6) + ((blockIdx.x >> 3) & 1), head = (blockIdx.x & 1) * 16 + (blockIdx.x >> 4);
  unsigned* req = sync + b * 32 * 33;
  unsigned* ack = req + 32;
  unsigned stale;
  ldw_sc1(stale, ack + head * 32, (lane & 31) * 4);
  const char* xb = (const char*)(x + (size_t)b * SQ * DM);
  const char* wqh = (const char*)(wq + head * DH);
  const char* kb = (const char*)(kc + (size_t)(b * NH + head) * SK * DH);
  const char* vb = (const char*)(vc + (size_t)(b * NH + head) * SK * DH);
  STAMP_DECL; STAMP(0); STAMPC(14);

  const int r7 = lane >> 3, sl = lane & 7;
  const unsigned swzV = 16 * (sl ^ (4 * ((r7 >> 1) & 1)));
  const unsigned voffW = (8 * w + r7) * (NH * DH * 2) + swzV;
  const unsigned voffV = (8 * w + r7) * 256 + swzV;
  const unsigned voffK = (8 * w + r7) * 256 + 16 * (sl ^ (4 * (w & 1) + (r7 >> 1)));
  const unsigned voffX0 = (32 * w + r7) * (DM * 2) + 16 * (sl ^ (r7 >> 1));
  const unsigned voffX1 = (32 * w + 8 + r7) * (DM * 2) + 16 * (sl ^ (4 + (r7 >> 1)));
  const unsigned xhi = (w == 7) ? 0u : 16u * DM * 2;
#define ROT 0
  const int rot = ROT;
  auto issueW = [&](int st0, unsigned dst) { const int st = (st0 + rot) & 7; const char* s = wqh + (size_t)st * 64 * (NH * DH * 2);
    dma<0>(dst + 2048 * w, s, voffW); dma<128>(dst + 2048 * w + 1024, s, voffW); };
  auto issueX = [&](int st0, unsigned dst) { const int st = (st0 + rot) & 7; const char* s = xb + st * 128; const unsigned d = dst + 4096 * w;
    dma<0>(d, s, voffX0); dma<0>(d + 1024, s, voffX1); dma<0>(d + 2048, s + xhi, voffX0); dma<0>(d + 3072, s + xhi, voffX1); };
#define ROTK 0
  const int rk = ROTK;
  auto kt = [&](int t) { return kb + ((t + rk) & 7) * U; };
  auto vt = [&](int t) { return vb + ((t + rk) & 7) * U; };
  auto issueK = [&](int t, unsigned dst) { const char* s = kt(t); dmas<0>(dst + 2048 * w, s, voffK); dmas<128>(dst + 2048 * w + 1024, s, voffK); };
  auto issueV = [&](int t, unsigned dst) { const char* s = vt(t); dmas<0>(dst + 2048 * w, s, voffV); dmas<128>(dst + 2048 * w + 1024, s, voffV); };
  issueW(0, lds0 + 4 * U); issueX(0, lds0); issueW(1, lds0 + 5 * U); issueX(1, lds0 + 2 * U);

  {
    const char* pc;
    asm volatile("s_getpc_b64 %0" : "=s"(pc));
    if (w < 3) touch(lds0 + 9 * U + 2048 * w, pc, min((w * 5 + min(lane, 4)) * 8 + ((blockIdx.x >> 3) & 7), CODELINES - 1) * 128);
    else if (w == 3) touch(lds0 + 9 * U + 2048 * w, wo, ((lane & 7) * 8 + ((blockIdx.x >> 3) & 7)) * 65536 + (blockIdx.x & 255) * 128);
    else if (w == 4) touch(lds0 + 9 * U + 2048 * w, wqh, ((lane & 7) * 8 + ((blockIdx.x >> 3) & 7)) * 65536);
    else if (w == 5) touch(lds0 + 9 * U + 2048 * w, ofrag + (size_t)b * 8 * RBS - 128, min(lane, 8) * RBS);
    else if (w == 6) { touch(lds0 + 9 * U + 2048 * w, kb, (lane & 1) * 4 * U); touch(lds0 + 9 * U + 2048 * w, vb, (lane & 1) * 4 * U); }
    else touch(lds0 + 9 * U + 2048 * w, out + (size_t)b * SQ * DM, (lane & 3) * 4 * U);
  }
  const int r = lane & 31, h = lane >> 5, g1 = (lane >> 4) & 1, q = (lane >> 2) & 3, p = lane & 3;
  unsigned rowb[4];
#pragma unroll
  for (int s = 0; s < 4; ++s) rowb[s] = 128 * (r & 7) + 16 * ((2 * s + h) ^ ((r >> 1) & 7));

  f32x16 acc[4] = {};
  {
    unsigned wbase[2];
#pragma unroll
    for (int d = 0; d < 2; ++d) wbase[d] = lds0 + 4 * U + 2048 * h + 128 * q + 16 * ((4 * d + 2 * g1 + (p & 1)) ^ (4 * (q >> 1))) + 8 * (p >> 1);
    const unsigned xbase = lds0 + 4096 * w + 1024 * (r >> 3);
    bf16x8 lf[4], lx;
    unsigned wa0 = wbase[0], wa1 = wbase[1], xa[4];
#pragma unroll
    for (int s = 0; s < 4; ++s) xa[s] = xbase + rowb[s];
    auto stage = [&](auto modec, int st, int wcur) {
      constexpr int mode = decltype(modec)::value;
      if (mode <= 1) WAITV(8); else if (mode == 2) WAITV(3);
      if (mode != 3) pin4(lf[0], lf[1], lf[2], lf[3]);
      BARRIER();
      bf16x8 xf[4], f[PD + 1];
      auto ldw = [&](int i) { const unsigned a = ((i & 1) ? wa1 : wa0) + 4096 * (i >> 2) + 1024 * ((i >> 1) & 1); return ldtr2(a, a + 512); };
#pragma unroll
      for (int i = 0; i < 4; ++i) {
        if (mode != 3) acc[i] = MFMA(lf[i], lx, acc[i]);
        xf[i] = ld128(xa[i]); f[i] = ldw(i);
        SBAR();
      }
      const char *xs, *ws, *kvs; unsigned xd, wd, kvd, kvo;
#pragma unroll
      for (int i = 0; i < 12; ++i) {
        if (i < 8) f[(i + PD) % (PD + 1)] = ldw(i + PD); else lf[i - 8] = ldw(i + PD);
        acc[i & 3] = MFMA(f[i % (PD + 1)], xf[i >> 2], acc[i & 3]);
        if (i == 0) { const int sn = (st + 2 + rot) & 7; xs = xb + sn * 128; ws = wqh + (size_t)sn * 64 * (NH * DH * 2); xd = lds0 + (st & 1) * 2 * U + 4096 * w; }
        if (i == 1) pin4(xf[0], xf[1], xf[2], xf[3]);
        if (mode == 0 || mode == 3) {
          if (i == 1) dma<0>(xd, xs, voffX0);
          if (i == 2) dma<0>(xd + 1024, xs, voffX1);
          if (i == 3) wd = lds0 + (4 + (wcur == 0 ? 2 : wcur - 1)) * U + 2048 * w;
          if (i == 4) dma<0>(xd + 2048, xs + xhi, voffX0);
          if (i == 5) dma<0>(xd + 3072, xs + xhi, voffX1);
          if (i == 6) { kvs = (st & 2) ? vt(0) : kt(st >> 2); kvd = lds0 + (7 + (st >> 1)) * U + 2048 * w + 1024 * (st & 1); kvo = ((st & 2) ? voffV : voffK) + 128 * (st & 1); }
          if (i == 7) dma<0>(wd, ws, voffW);
          if (i == 8) dma<128>(wd + 1024, ws, voffW);
          if (i == 10) dmas<0>(kvd, kvs, kvo);
        } else if (mode == 1) {
          if (i == 3) dmas<0>(lds0 + 6 * U + 2048 * w, vt(1), voffV);
          if (i == 9) dmas<128>(lds0 + 6 * U + 2048 * w + 1024, vt(1), voffV);
        } else {
          if (i == 1) dmas<0>(lds0 + 2048 * w, kt(2), voffK);
          if (i == 4) dmas<128>(lds0 + 2048 * w + 1024, kt(2), voffK);
          if (i == 7) dmas<0>(lds0 + U + 2048 * w, vt(2), voffV);
          if (i == 10) dmas<128>(lds0 + U + 2048 * w + 1024, vt(2), voffV);
        }
        if (i == 9) { xa[0] ^= 2 * U; xa[1] ^= 2 * U; xa[2] ^= 2 * U; xa[3] ^= 2 * U; OPAQUE2(xa[0], xa[1]); OPAQUE2(xa[2], xa[3]); }
        SBAR();
      }
      lx = xf[3];
      const unsigned dw = wcur == 2 ? -2 * U : U;
      wa0 += dw; wa1 += dw; OPAQUE2(wa0, wa1);
    };
    waitv1<7>(stale);
    const unsigned nonce = ~(unsigned)__ballot((stale >> (lane & 31)) & 1);
    if (w == 0 && lane == 0) stw_sc1(req + head, 0, nonce);
    stage(IC<3>{}, 0, 0);
    int wcur = 1;
#pragma unroll 1
    for (int st = 1; st < 6; ++st) { stage(IC<0>{}, st, wcur); wcur = wcur == 2 ? 0 : wcur + 1; }
    stage(IC<1>{}, 6, 0);
    stage(IC<2>{}, 7, 1);
#pragma unroll
    for (int i = 0; i < 4; ++i) acc[i] = MFMA(lf[i], lx, acc[i]);
  }
  STAMP(1);
  bf16x8 qf[8];
  float qs = QSCALE;
  asm volatile("s_nop 7\n\ts_nop 7" : "+v"(acc[0]), "+v"(acc[1]), "+v"(acc[2]), "+v"(acc[3]), "+v"(qs));
#pragma unroll
  for (int d = 0; d < 4; ++d) { for (int i = 0; i < 16; ++i) acc[d][i] = fmul(acc[d][i], qs); qf[2 * d] = pack8<0>(acc[d]); qf[2 * d + 1] = pack8<1>(acc[d]); }

  unsigned kaddr[4], vaddr[2];
#pragma unroll
  for (int s = 0; s < 4; ++s) kaddr[s] = lds0 + 2048 * (r >> 3) + rowb[s];
#pragma unroll
  for (int d = 0; d < 2; ++d) vaddr[d] = lds0 + 128 * (4 * h + q) + 16 * ((4 * d + 2 * g1 + (p >> 1)) ^ (4 * (q >> 1))) + 8 * (p & 1);
  f32x16 o[4] = {}, s0, s1, negm = {};
  float l0 = 0.f, l1 = 0.f, l2 = 0.f, l3 = 0.f;
  const bool grpB = w >= 4;
  const int rp = head & 3, cb = head >> 2;
#define KROT (16 * b + 4 * rp)
  const int krot = KROT;
  const unsigned voffWo = r7 * (DM * 2) + swzV;
  const char* wos = (const char*)(wo + (size_t)(512 * w) * DM + cb * 64);
  auto kstep = [&](int u) { return (u + krot) & 31; };
  auto issueWo = [&](int u, unsigned unit) { const char* s = wos + (size_t)kstep(u) * 16 * (DM * 2); const unsigned d = lds0 + unit * U + 2048 * w;
    DMAWO<0>(d, s, voffWo); DMAWO<0>(d + 1024, s + 8 * DM * 2, voffWo); };
  bf16x8 f[PD + 1];
  unsigned ka[4];
  auto ldk = [&](int i) { return ld128(ka[(i >> 1) & 3] + 1024 * (i >> 3) + 8192 * (i & 1)); };
#pragma unroll
  for (int s = 0; s < 4; ++s) ka[s] = kaddr[s] + 7 * U;
#pragma unroll
  for (int i = 0; i < PD; ++i) f[i] = ldk(i);
#pragma unroll 1
  for (int t = 0; t < 8; ++t) {
    STT(9);
    SBAR();
#pragma unroll
    for (int i = 0; i < 16; ++i) {
      if (i + PD < 16) f[(i + PD) % (PD + 1)] = ldk(i + PD);
      if (i & 1) s1 = MFMA(f[i % (PD + 1)], qf[i >> 1], i < 2 ? negm : s1);
      else s0 = MFMA(f[i % (PD + 1)], qf[i >> 1], i < 2 ? negm : s0);
      SBAR();
    }
    auto sync = [&]() {
      if (t <= 5) WAITV(4); else if (t == 6) WAITV(6);
      BARRIER();
    };
    STT(10);
    SBAR();
    if (grpB) sync();
    SBAR();
    STT(11);
    if (APRIO) __builtin_amdgcn_s_setprio(APRIO == 1 ? 0 : 1);
    asm volatile("s_nop 7\n\ts_nop 7" : "+v"(s0), "+v"(s1));
    float m0 = max3(s0[0], s0[1], s0[2]), m1 = max3(s0[8], s0[9], s0[10]), m2 = max3(s1[0], s1[1], s1[2]), m3 = max3(s1[8], s1[9], s1[10]);
    m0 = max3(m0, s0[3], s0[4]); m1 = max3(m1, s0[11], s0[12]); m2 = max3(m2, s1[3], s1[4]); m3 = max3(m3, s1[11], s1[12]);
    m0 = max3(m0, s0[5], s0[6]); m1 = max3(m1, s0[13], s0[14]); m2 = max3(m2, s1[5], s1[6]); m3 = max3(m3, s1[13], s1[14]);
    m0 = max3(m0, s0[7], s0[15]); m2 = max3(m2, s1[7], s1[15]);
    m0 = max3(m0, m1, m3);
    const float mx = max3(m0, m2, m2);
    if (t == 0 || __any(mx > THR)) {
      float d = fmaxf(mx, xhalf(mx));
      if (t != 0) {
        d = fmaxf(d, 0.f);
        const float alpha = __builtin_amdgcn_exp2f(-d);
        l0 *= alpha; l1 *= alpha; l2 *= alpha; l3 *= alpha;
#pragma unroll
        for (int i = 0; i < 4; ++i) o[i] *= alpha;
      }
      s0 -= d; s1 -= d; negm -= d;
    }
    const unsigned vo = ((VUNITS >> (4 * t)) & 15) * U, ko = (((KUNITS >> 4) >> (4 * t)) & 15) * U;
    unsigned va0 = vaddr[0] + vo, va1 = vaddr[1] + vo;
    OPAQUE2(va0, va1);
    auto ldv = [&](int i) { const unsigned a = ((i & 1) ? va1 : va0) + 4096 * (i >> 2) + 1024 * ((i >> 1) & 1); return ldtr2(a, a + 2048); };
    bf16x8 g[PD + 1];
    u32x4 pw[4];
#pragma unroll
    for (int i = 0; i < 4; ++i) { exp4(s0[4 * i], s0[4 * i + 1], s0[4 * i + 2], s0[4 * i + 3], l0, l1, l2, l3, pw[i >> 1], i & 1); }
#pragma unroll
    for (int i = 0; i < 4; ++i) { exp4(s1[4 * i], s1[4 * i + 1], s1[4 * i + 2], s1[4 * i + 3], l0, l1, l2, l3, pw[2 + (i >> 1)], i & 1);
      if (i == 1) { SBAR();
#pragma unroll
        for (int j = 0; j < PD; ++j) g[j] = ldv(j);
        SBAR(); } }
    bf16x8 pf[4];
#pragma unroll
    for (int i = 0; i < 4; ++i) pf[i] = __builtin_bit_cast(bf16x8, pw[i]);
#pragma unroll
    for (int s = 0; s < 4; ++s) ka[s] = kaddr[s] + ko;
    OPAQUE2(ka[0], ka[1]); OPAQUE2(ka[2], ka[3]);
    const bool kv = t <= 4;
    const int j0 = 3 * (t - 5);
    const char* sa = (kv ? kb : wos) + (kv ? ((t + 3 + rk) & 7) : kstep(j0)) * U;
    const char* sb = (kv ? vb : wos) + (kv ? ((t + 3 + rk) & 7) : kstep(j0 + 1)) * U;
    unsigned da = lds0 + 2048 * w + ((AUNITS >> (4 * t)) & 15) * U, db = lds0 + 2048 * w + ((BUNITS >> (4 * t)) & 15) * U;
    const unsigned oa = kv ? voffK : voffWo, ob = kv ? voffV : voffWo, second = kv ? 128 : 8 * DM * 2;
    asm volatile("" : "+s"(sa), "+s"(sb), "+s"(da), "+s"(db));
    STT(12);
    if (APRIO) __builtin_amdgcn_s_setprio(APRIO == 1 ? 1 : 0);
    SBAR();
    if (!grpB) sync();
    SBAR();
    STT(13);
#pragma unroll
    for (int i = 0; i < 16; ++i) {
      if (i + PD < 16) g[(i + PD) % (PD + 1)] = ldv(i + PD); else f[i + PD - 16] = ldk(i + PD - 16);
      o[i & 3] = MFMA(g[i % (PD + 1)], pf[i >> 2], o[i & 3]);
      if (i == 1) dma<0>(da, sa, oa);
      if (i == 4) dma<0>(da + 1024, sa + second, oa);
      if (i == 7) dma<0>(db, sb, ob);
      if (i == 10) dma<0>(db + 1024, sb + second, ob);
      if (i == 13) { if (!kv) issueWo(j0 + 2, (unsigned)(WUNITS >> (4 * j0 + 8)) & 15); }
      SBAR();
    }
    STT(14);
  }
  STAMP(2);
  if (APRIO) __builtin_amdgcn_s_setprio(0);
  unsigned reqv;
  ldw_sc1(reqv, req, (lane & 31) * 4);
  const float lsum = (l0 + l1) + (l2 + l3), rl = 1.0f / (lsum + xhalf(lsum));
  {
    asm volatile("s_nop 7\n\ts_nop 7" : "+v"(o[0]), "+v"(o[1]), "+v"(o[2]), "+v"(o[3]));
    const char* dst = ofrag + (size_t)(b * 8 + w) * RBS + head * 8192;
#pragma unroll
    for (int d = 0; d < 4; ++d) {
      for (int i = 0; i < 16; ++i) o[d][i] = fmul(o[d][i], rl);
      st_sc1<0>(dst + 2048 * d, lane * 16, pack8<0>(o[d]));
      st_sc1<1024>(dst + 2048 * d, lane * 16, pack8<1>(o[d]));
    }
  }
  STAMP(3);
  unsigned wa[2];
#pragma unroll
  for (int d = 0; d < 2; ++d) wa[d] = vaddr[d] + 2048 * w;
  auto ldwo = [&](int j, int n) { const unsigned a = wa[n] + ((unsigned)(WUNITS >> (4 * j)) & 15) * U; return ldtr2(a, a + 1024); };
  const unsigned* ackw = ack + 4 * w * 32 + head;
  pins(ackw);
  bf16x8 wr[18][2];
  WAITV(9);
#pragma unroll
  for (int j = 0; j < 9; ++j) { wr[j][0] = ldwo(j, 0); wr[j][1] = ldwo(j, 1); }
#pragma unroll
  for (int j = 0; j < 9; ++j) { pin2(wr[j][0], wr[j][1]); if (j < PUBN) issueWo(9 + j, (unsigned)(WUNITS >> (4 * j)) & 15); }
  waitv1<2 * PUBN>(reqv);
  BARRIER();
  if (w == 0 && lane < 32) stw_sc1(ack + head, lane * 128, reqv);
  STAMP2(4);
#pragma unroll
  for (int j = PUBN; j < 9; ++j) issueWo(9 + j, (unsigned)(WUNITS >> (4 * j)) & 15);
  issueWo(27, 1);
  static_for<9>([&](auto jc) { constexpr int j = decltype(jc)::value;
    asm volatile("s_waitcnt vmcnt(18)" ::: "memory");
    wr[9 + j][0] = ldwo(j, 0); wr[9 + j][1] = ldwo(j, 1);
    pin2(wr[9 + j][0], wr[9 + j][1]); issueWo(18 + j, (unsigned)(WUNITS >> (4 * j)) & 15); });
  STAMP2(5);
  {
    const unsigned nonce = __builtin_amdgcn_readlane(reqv, head);
    const unsigned long long need = (15ull << (4 * w)) & ~(1ull << head);
    for (;;) {
      unsigned v;
      asm volatile("global_load_dword %0, %1, %2 sc1\n\ts_waitcnt vmcnt(0)" : "=&v"(v) : "v"((lane & 31) * 4), "s"(ack + head * 32) : "memory");
      if ((__ballot(v == nonce) & need) == need) break;
      __builtin_amdgcn_s_sleep(2);
    }
  }
  STAMP2(6);
  f32x16 pa[4] = {};
  {
    const char* os = ofrag + (size_t)(b * 8 + 2 * rp) * RBS + 4 * w * 8192;
    bf16x8 of[32][2];
    auto ldo = [&](auto nc) { constexpr int n = decltype(nc)::value; const char* s = os + kstep(seq(n)) * 1024;
      ld_sc1(of[n][0], s, lane * 16); ld_sc1(of[n][1], s + RBS, lane * 16); };
    static_for<issued(0)>(ldo);
    bf16x8 wl[2][2];
    unsigned reqv2;
    static_for<32>([&](auto nc) { constexpr int n = decltype(nc)::value; constexpr int u = seq(n), un = seq(n + 1);
      waitv2<inflight(n)>(of[n][0], of[n][1]);
      if constexpr (n < 31 && un >= 18) { constexpr int j = un >= 28 ? un - 28 : un - 18; wl[(n + 1) & 1][0] = ldwo(j, 0); wl[(n + 1) & 1][1] = ldwo(j, 1); }
      SBAR();
      const bf16x8 a0 = u < 18 ? wr[u < 18 ? u : 0][0] : wl[n & 1][0], a1 = u < 18 ? wr[u < 18 ? u : 0][1] : wl[n & 1][1];
      pa[0] = MFMA(a0, of[n][0], pa[0]); pa[1] = MFMA(a1, of[n][0], pa[1]);
      SBAR();
      if constexpr (n == 14) ldw_sc1(reqv2, req + 4 * w, (lane & 3) * 4);
      if constexpr (n == 24) { waitv1<63>(reqv2); stw_sc1(ackw, (lane & 3) * 128, reqv2); }
      if constexpr (n >= 8 && n < 12) issueWo(20 + n, (unsigned)(WUNITS >> (4 * (n & 3))) & 15);
      static_for<issued(n + 1) - issued(n)>([&](auto mc) { ldo(IC<issued(n) + decltype(mc)::value>{}); });
      SBAR();
      pa[2] = MFMA(a0, of[n][1], pa[2]); pa[3] = MFMA(a1, of[n][1], pa[3]);
      SBAR();
    });
  }
  STAMP2(7);
  int lane2;
  asm volatile("v_mbcnt_lo_u32_b32 %0, -1, 0\n\tv_mbcnt_hi_u32_b32 %0, -1, %0" : "=v"(lane2));
  const int sl2 = lane2 & 7;
#pragma unroll
  for (int R = 0; R < 2; ++R)
#pragma unroll
    for (int c = 0; c < 8; ++c) {
      const f32x16& a = pa[2 * R + (c >> 2)]; const int g = c & 3;
      f32x4 v = {a[4 * g], a[4 * g + 1], a[4 * g + 2], a[4 * g + 3]};
      *(LDSP(f32x4))(lds0 + (4 * R + (c >> 1)) * U + 2048 * w + 1024 * (c & 1) + 16 * (lane2 ^ (2 * c))) = v;
    }
  WAITL0(); BARRIER();
  const int row = 8 * w + (lane2 >> 3);
  const unsigned ra = lds0 + (4 * (row >> 5) + (sl2 >> 1)) * U + 1024 * (sl2 & 1) + 16 * ((row & 31) ^ (2 * sl2));
  f32x4 lo = {}, hi = {};
#pragma unroll
  for (int i = 0; i < 8; ++i) { lo += *(LDSP(f32x4))(ra + 2048 * i); hi += *(LDSP(f32x4))(ra + 2048 * i + 512); }
  const int grow = 64 * rp + row;
  if (grow < SQ) {
    u32x4 v = {pk2(lo[0], lo[1]), pk2(lo[2], lo[3]), pk2(hi[0], hi[1]), pk2(hi[2], hi[3])};
    asm volatile("global_store_dwordx4 %0, %1, %2" OUTPOL "\n\ts_nop 1" :: "v"((grow * DM + 8 * sl2) * 2), "v"(v), "s"(out + (size_t)b * SQ * DM + cb * 64) : "memory");
  }
  STAMP2(8); STAMPC(15); STAMP_OUT(0, 15);
}

extern "C" void kernel_launch(void* const* d_in, const int* in_sizes, int n_in, void* d_out, int out_size, void* d_ws, size_t ws_size,
                              hipStream_t stream) {
  (void)hipFuncSetAttribute((const void*)attn_kernel, hipFuncAttributeMaxDynamicSharedMemorySize, 163840);
  attn_kernel<<<256, 512, 163840, stream>>>((const __bf16*)d_in[0], (const __bf16*)d_in[1], (const __bf16*)d_in[2], (const __bf16*)d_in[3], (const __bf16*)d_in[4],
                                            (__bf16*)d_out, (char*)d_ws + 65536, (unsigned*)d_ws);
}
```
